```python
import torch

def _softmax(x, axis=-1):
    return torch.softmax(x, dim=axis)
def _gelu(x):
    return torch.nn.functional.gelu(x, approximate="tanh")
def _silu(x):
    return torch.nn.functional.silu(x)
def _sigmoid(x):
    return torch.sigmoid(x)
def _layernorm(x, axis=-1, eps=1e-5):
    m = x.mean(dim=axis, keepdim=True)
    v = x.var(dim=axis, keepdim=True, unbiased=False)
    return (x - m) / torch.sqrt(v + eps)
def _scatter_add(tgt, idx, src, axis=0):
    return tgt.clone().index_add_(axis, idx, src)
def _segment_sum(src, seg, n):
    out = torch.zeros((n,) + src.shape[1:], dtype=src.dtype)
    return out.index_add_(0, seg, src)
def _pad(x, pw):
    flat = tuple(v for pair in reversed(pw) for v in pair)
    return torch.nn.functional.pad(x, flat)

kernel_name = 'standalone__depthwise_separable__vision'

def setup_inputs():

    _w = (lambda a: (lambda t: t.to(torch.bfloat16) if t.is_floating_point() else t)(torch.as_tensor(a)))
    H = W = 512
    C = 192
    k = 1
    return {
        "x": _w((lambda *s: torch.randn(*s, dtype=torch.bfloat16) * 0.05)(1, H, W, C)),
        "dw": _w((lambda *s: torch.randn(*s, dtype=torch.bfloat16) * 0.05)(k, k, C)),
        "pw": _w((lambda *s: torch.randn(*s, dtype=torch.bfloat16) * 0.05)(C, C)),
    }

def reference(x, dw, pw):
    B, H, W, C = x.shape
    k = dw.shape[0]
    pad = k // 2
    xp = _pad(x, ((0,0),(pad,pad),(pad,pad),(0,0)))
    out = torch.zeros_like(x)
    for i in range(k):
        for j in range(k):
            out = out + xp[:, i:i+H, j:j+W, :] * dw[i, j][None, None, None, :]
    return torch.einsum('bhwc,cd->bhwd', out, pw)
```

```cpp
#include <hip/hip_runtime.h>
#include <hip/hip_bf16.h>
#include <stdint.h>

#define LD_AUX 2
#define ST_AUX 16
#define NBLK 256
#define DEPTH 3
#define NSLOT 6
#define EXTRA 0
#define NSTAT (2 * DEPTH + 2)
#define PSZ 16
#define CH 32
#define IPF 2
#define RLIM 3
#define RSW 12
#define PA 2
#define WB 1
#define WPF 3
#define CGROT 1
#define PPRIO 2

typedef __attribute__((ext_vector_type(8))) __bf16 bf16x8;
typedef __attribute__((ext_vector_type(2))) __bf16 bf16x2;
typedef __attribute__((ext_vector_type(4))) float f32x4;
typedef __attribute__((ext_vector_type(2))) float f32x2;
typedef __attribute__((ext_vector_type(4))) unsigned int u32x4;
typedef __attribute__((ext_vector_type(2))) unsigned int u32x2;
typedef __attribute__((address_space(3))) volatile int lds_vint;
typedef __attribute__((address_space(3))) volatile unsigned long long lds_vu64;
typedef __attribute__((address_space(3))) volatile u32x4 lds_vu4;

constexpr int C = 192;
constexpr int ROWB = C * 2;
constexpr int TPX = 16;
constexpr int TILEB = TPX * ROWB;
constexpr int TSL = TILEB / 16;
constexpr unsigned OOB = 0xF0000000u;
constexpr int WSLOTS = 72 * 64;
constexpr int NCONS = 3;
constexpr int RING0 = WSLOTS;
constexpr int OSTG0 = RING0 + 2 * NSLOT * TSL;
constexpr int FLAG0 = OSTG0 + 2 * NCONS * 128;
constexpr int NFLAG = 64;
static_assert(WB >= 1 && WB <= DEPTH && PA >= 1 && PA <= DEPTH && RLIM < NSLOT && RSW >= NSLOT && NSTAT >= 2 * DEPTH + EXTRA && NSLOT <= 8 && NSLOT >= NCONS, "");
constexpr int SPINMAX = 1 << 22;

__device__ __forceinline__ unsigned pack_bf16(float a, float b) {
    f32x2 v = {a, b};
    bf16x2 r = __builtin_convertvector(v, bf16x2);
    return __builtin_bit_cast(unsigned, r);
}
__device__ __forceinline__ float lo_f(unsigned u) { return __builtin_bit_cast(float, u << 16); }
__device__ __forceinline__ float hi_f(unsigned u) { return __builtin_bit_cast(float, u & 0xffff0000u); }
__device__ __forceinline__ void wait_ge(lds_vint* f, int v) {
#pragma unroll 1
    for (int i = 0; i < SPINMAX; ++i) {
        if (__builtin_amdgcn_readfirstlane(*f) >= v) break;
        __builtin_amdgcn_s_sleep(1);
    }
    asm volatile("" ::: "memory");
}

__device__ __forceinline__ void wait_freed(lds_vint* f, int v) {
#pragma unroll 1
    for (int i = 0; i < SPINMAX; ++i) {
        const u32x4 e = *(lds_vu4*)f;
        if (__builtin_amdgcn_readfirstlane(min(min((int)e[0], (int)e[1]), (int)e[2])) >= v) break;
        __builtin_amdgcn_s_sleep(1);
    }
    asm volatile("" ::: "memory");
}

__global__ __launch_bounds__(512, 2)
void dwsep_kernel(const uint16_t* __restrict__ x, const uint16_t* __restrict__ dw,
                  const uint16_t* __restrict__ pw, uint16_t* __restrict__ out,
                  unsigned* __restrict__ qctr, int ntiles, unsigned nbytes
                  )
{
    __shared__ __attribute__((aligned(16))) u32x4 lds[FLAG0 + NFLAG / 4];
    const int tid = threadIdx.x;
    unsigned ipf;
    {
        unsigned long long pc; int len;
        asm volatile("s_getpc_b64 %0\ndwsep_code_a:\n\ts_mov_b32 %1, dwsep_code_b-dwsep_code_a" : "=s"(pc), "=s"(len));
        const __amdgpu_buffer_rsrc_t rc = __builtin_amdgcn_make_buffer_rsrc((void*)pc, 0, len > 0 ? len : 0, 0x00020000);
        ipf = __builtin_amdgcn_raw_buffer_load_b32(rc, (unsigned)(tid - IPF * 64) * 128u, 0, 0);
        __builtin_amdgcn_sched_barrier(0);
    }
    unsigned wpf;
    {
        const __amdgpu_buffer_rsrc_t rw = __builtin_amdgcn_make_buffer_rsrc((void*)pw, 0, C * C * 2, 0x00020000);
        const unsigned o = (tid >> 6) == WPF ? ((blockIdx.x >> 3) % 9) * 8192u + (tid & 63) * 128u : OOB;
        wpf = __builtin_amdgcn_raw_buffer_load_b32(rw, o, 0, 0);
        __builtin_amdgcn_sched_barrier(0);
    }
    const int lane = tid & 63;
    const int wave = __builtin_amdgcn_readfirstlane(tid >> 6);
    const int team = wave & 1;
    const int p = lane & 15, g = lane >> 4;
    const int r8 = lane >> 3, c8 = lane & 7;
    const unsigned voff = r8 * ROWB + c8 * 16;
    lds_vint* const flags = (lds_vint*)(lds + FLAG0);
    lds_vu64* const full = (lds_vu64*)(flags + team * 28);
    lds_vint* const freed = flags + team * 28 + 16;
    lds_vint* const wready = flags + 56;
    u32x4* const ring = lds + RING0 + team * NSLOT * TSL;

    if (wave < 2) {
        const __amdgpu_buffer_rsrc_t rx = __builtin_amdgcn_make_buffer_rsrc((void*)x, 0, nbytes, 0x00020000);
        const int nq = (gridDim.x + PSZ - 1) / PSZ;
        const int qid = blockIdx.x / PSZ;
        const int tp = 2 * min(PSZ, (int)gridDim.x - qid * PSZ);
        const int ti = (blockIdx.x % PSZ) * 2 + team;
        unsigned* const ctr = qctr + qid * 64;
        if (ti == 0 && lane == 0) __hip_atomic_store(ctr, (unsigned)(NSTAT * tp), __ATOMIC_RELAXED, __HIP_MEMORY_SCOPE_AGENT);
        auto qtile = [&](const unsigned k) __attribute__((always_inline)) -> int {
            const unsigned long long t = (unsigned long long)(k / CH) * (unsigned)(CH * nq) + (unsigned)(qid * CH) + k % CH;
            return t < (unsigned long long)ntiles ? (int)t : -1;
        };
        __builtin_amdgcn_sched_barrier(0);
        u32x4 dwr[3];
#pragma unroll
        for (int j = 0; j < 3; ++j) dwr[j] = *(const u32x4*)(dw + j * 64 + c8 * 8);
        u32x4 xr[DEPTH][6];
        int tl[DEPTH];
        unsigned kq[DEPTH];
        bool pend[DEPTH];
#pragma unroll
        for (int d = 0; d < DEPTH; ++d) {
            tl[d] = qtile((EXTRA + d) * tp + ti);
            kq[d] = 0; pend[d] = false;
            const unsigned so = ((tl[d] >= 0) ? (unsigned)tl[d] * TILEB : OOB) + voff;
#pragma unroll
            for (int u = 0; u < 6; ++u)
                xr[d][u] = __builtin_amdgcn_raw_buffer_load_b128(rx, so + (u / 3) * 8 * ROWB + (u % 3) * 128, 0, LD_AUX);
            if (d == WB - 1) {
                __builtin_amdgcn_sched_barrier(0);
                if (tid < NFLAG) flags[tid] = 0;
                asm volatile("s_waitcnt lgkmcnt(0)\n\ts_barrier" ::: "memory");
                __builtin_amdgcn_sched_barrier(0);
            }
        }
        __builtin_amdgcn_s_setprio(PPRIO);
        float dwf[3][8];
#pragma unroll
        for (int j = 0; j < 3; ++j)
#pragma unroll
            for (int k = 0; k < 4; ++k) {
                dwf[j][2 * k] = lo_f(dwr[j][k]); dwf[j][2 * k + 1] = hi_f(dwr[j][k]);
                asm volatile("" : "+v"(dwf[j][2 * k]), "+v"(dwf[j][2 * k + 1]));
            }
        asm volatile("" :: "v"(ipf));
        asm volatile("" :: "v"(wpf));
        const int iw0 = r8 * 8 + (c8 ^ (r8 >> 1));
        const int iw1 = (8 + r8) * 8 + (c8 ^ (4 + (r8 >> 1)));
        int slot = 0, n = 0;
        auto post = [&](const u32x4 (&xs)[6], const int tile) __attribute__((always_inline)) {
            if (n >= NSLOT) wait_freed(freed, n + 1 - ((RLIM && n >= RSW) ? RLIM : NSLOT));
            u32x4* sb = ring + slot * TSL;
#pragma unroll
            for (int u = 0; u < 6; ++u) {
                const int j = u % 3;
                u32x4 v = xs[u];
#pragma unroll
                for (int k = 0; k < 4; ++k)
                    v[k] = pack_bf16(lo_f(v[k]) * dwf[j][2 * k], hi_f(v[k]) * dwf[j][2 * k + 1]);
                sb[j * 128 + ((u / 3) ? iw1 : iw0)] = v;
            }
            asm volatile("" ::: "memory");
            full[slot] = ((unsigned long long)(unsigned)tile << 32) | (unsigned)(n + 1);
            slot = (slot + 1 == NSLOT) ? 0 : slot + 1;
            ++n;
        };
        bool stop = false, live = true;
#pragma unroll 1
        for (int st0 = EXTRA; live; st0 += DEPTH) {
            live = false;
#pragma unroll
            for (int d = 0; d < DEPTH; ++d) {
                const int st = st0 + d;
                if (tl[d] >= 0) post(xr[d], tl[d]);
                __builtin_amdgcn_sched_barrier(0);
                int nt = -1;
                if (st + DEPTH < NSTAT) {
                    nt = qtile((st + DEPTH) * tp + ti);
                } else if (pend[d]) {
                    nt = qtile(__builtin_amdgcn_readfirstlane(kq[d]));
                    if (nt < 0) stop = true;
                }
                const int dp = (d + PA) % DEPTH;
                pend[dp] = (st + PA + DEPTH >= NSTAT) && !stop;
                if (pend[dp] && lane == 0) {
                    int z = 0;
                    asm volatile("" : "+v"(z));
                    kq[dp] = __hip_atomic_fetch_add(ctr + z, 1u, __ATOMIC_RELAXED, __HIP_MEMORY_SCOPE_AGENT);
                }
                __builtin_amdgcn_sched_barrier(0);
                tl[d] = nt;
                live = live || pend[dp] || nt >= 0 || st + PA + DEPTH < NSTAT;
                const unsigned so = ((nt >= 0) ? (unsigned)nt * TILEB : OOB) + voff;
#pragma unroll
                for (int u = 0; u < 6; ++u)
                    xr[d][u] = __builtin_amdgcn_raw_buffer_load_b128(rx, so + (u / 3) * 8 * ROWB + (u % 3) * 128, 0, LD_AUX);
                __builtin_amdgcn_sched_barrier(0);
            }
        }
        asm volatile("dwsep_code_b:");
        if (n >= NSLOT) wait_freed(freed, n + 1 - NSLOT);
        full[slot] = 0xFFFFFFFF00000000ull | (unsigned)(n + 1);
    } else {
        const __amdgpu_buffer_rsrc_t ro = __builtin_amdgcn_make_buffer_rsrc((void*)out, 0, nbytes, 0x00020000);
        const int cg = (((wave - 2) >> 1) + (CGROT ? blockIdx.x >> 3 : 0)) % 3;
        {
            const int tk = tid & 127;
            const int cb = tk >> 3, db = cg * 8 + (tk & 7);
            const int hf = tk & 1;
            const int cb2 = 16 + (tk >> 4), db2 = cg * 8 + ((tk >> 1) & 7);
            u32x4 wr[8];
            u32x2 wh[8];
            asm volatile("s_barrier" ::: "memory");
            __builtin_amdgcn_sched_barrier(0);
#pragma unroll
            for (int j = 0; j < 8; ++j) wr[j] = *(const u32x4*)(pw + (cb * 8 + j) * C + db * 8);
#pragma unroll
            for (int j = 0; j < 8; ++j) wh[j] = *(const u32x2*)(pw + (cb2 * 8 + j) * C + db2 * 8 + hf * 4);
            __builtin_amdgcn_sched_barrier(0);
#pragma unroll
            for (int e = 0; e < 8; ++e) {
                u32x4 v;
                const unsigned sel = (e & 1) ? 0x07060302u : 0x05040100u;
#pragma unroll
                for (int jj = 0; jj < 4; ++jj)
                    v[jj] = __builtin_amdgcn_perm(wr[2 * jj + 1][e >> 1], wr[2 * jj][e >> 1], sel);
                lds[((2 * (db >> 2) + (e >> 2)) * 6 + (cb >> 2)) * 64 + (cb & 3) * 16 + 4 * (db & 3) + (e & 3)] = v;
            }
#pragma unroll
            for (int e = 0; e < 4; ++e) {
                u32x4 v;
                const unsigned sel = (e & 1) ? 0x07060302u : 0x05040100u;
#pragma unroll
                for (int jj = 0; jj < 4; ++jj)
                    v[jj] = __builtin_amdgcn_perm(wh[2 * jj + 1][e >> 1], wh[2 * jj][e >> 1], sel);
                lds[((2 * (db2 >> 2) + hf) * 6 + (cb2 >> 2)) * 64 + (cb2 & 3) * 16 + 4 * (db2 & 3) + e] = v;
            }
            asm volatile("" ::: "memory");
            if (lane == 0) __hip_atomic_fetch_add((int*)(lds + FLAG0) + 56 + cg, 1, __ATOMIC_RELAXED, __HIP_MEMORY_SCOPE_WORKGROUP);
            asm volatile("" :: "v"(ipf));
        asm volatile("" :: "v"(wpf));
            wait_ge(wready + cg, 2);
        }
        u32x4 wf[24];
#pragma unroll
        for (int f = 0; f < 24; ++f) wf[f] = lds[(cg * 24 + f) * 64 + lane];
        u32x4* const stg = lds + OSTG0 + (wave - 2) * 128;
        const int ir0 = p * 8 + (g ^ (p >> 1));
        const int ir1 = p * 8 + ((4 + g) ^ (p >> 1));
        const int ow0 = p * 8 + (g ^ (p & 7));
        const int ow1 = p * 8 + ((4 + g) ^ (p & 7));
        const int or0 = r8 * 8 + (c8 ^ r8);
        int slot = 0;
#pragma unroll 1
        for (int n = 1;; ++n) {
            int tile = -1;
#pragma unroll 1
            for (int i = 0; i < SPINMAX; ++i) {
                const unsigned long long e = full[slot];
                if (__builtin_amdgcn_readfirstlane((int)(unsigned)e) == n) { tile = __builtin_amdgcn_readfirstlane((int)(e >> 32)); break; }
                __builtin_amdgcn_s_sleep(1);
            }
            asm volatile("" ::: "memory");
            if (tile < 0) break;
            const u32x4* sb = ring + slot * TSL;
            u32x4 xa[6];
#pragma unroll
            for (int j = 0; j < 3; ++j) { xa[2 * j] = sb[j * 128 + ir0]; xa[2 * j + 1] = sb[j * 128 + ir1]; }
            asm volatile("" ::: "memory");
            freed[cg] = n;
            slot = (slot + 1 == NSLOT) ? 0 : slot + 1;
            const unsigned so = (unsigned)tile * TILEB + voff + cg * 128;
            f32x4 acc[4];
#pragma unroll
            for (int h = 0; h < 4; ++h) acc[h] = (f32x4){0.f, 0.f, 0.f, 0.f};
#pragma unroll
            for (int s = 0; s < 6; ++s)
#pragma unroll
                for (int h = 0; h < 4; ++h)
                    acc[h] = __builtin_amdgcn_mfma_f32_16x16x32_bf16(__builtin_bit_cast(bf16x8, wf[h * 6 + s]),
                                                                     __builtin_bit_cast(bf16x8, xa[s]), acc[h], 0, 0, 0);
#pragma unroll
            for (int m = 0; m < 2; ++m) {
                u32x4 o;
                o[0] = pack_bf16(acc[2 * m][0], acc[2 * m][1]);
                o[1] = pack_bf16(acc[2 * m][2], acc[2 * m][3]);
                o[2] = pack_bf16(acc[2 * m + 1][0], acc[2 * m + 1][1]);
                o[3] = pack_bf16(acc[2 * m + 1][2], acc[2 * m + 1][3]);
                stg[m ? ow1 : ow0] = o;
            }
#pragma unroll
            for (int q = 0; q < 2; ++q) {
                const u32x4 v = stg[q * 64 + or0];
                __builtin_amdgcn_raw_buffer_store_b128(v, ro, so + q * 8 * ROWB, 0, ST_AUX);
            }
        }
    }
}

extern "C" void kernel_launch(void* const* d_in, const int* in_sizes, int n_in,
                              void* d_out, int out_size, void* d_ws, size_t ws_size,
                              hipStream_t stream) {
    const uint16_t* x = (const uint16_t*)d_in[0];
    const uint16_t* dw = (const uint16_t*)d_in[1];
    const uint16_t* pw = (const uint16_t*)d_in[2];
    uint16_t* out = (uint16_t*)d_out;
    const int npix = in_sizes[0] / C;
    const int ntiles = (npix + TPX - 1) / TPX;
    int grid = (ntiles + 1) / 2;
    if (grid > NBLK) grid = NBLK;
    unsigned* qctr = (unsigned*)d_ws + (ws_size >= (size_t)(8u << 20) ? (1 << 20) : 0);
    dwsep_kernel<<<grid, 512, 0, stream>>>(x, dw, pw, out, qctr, ntiles, (unsigned)npix * ROWB);
}
```

```cpp
#include <hip/hip_runtime.h>
#include <hip/hip_bf16.h>
#include <stdint.h>

#define LD_AUX 2
#define ST_AUX 16
#define NBLK 256
#define DEPTH 3
#define NSLOT 6
#define EXTRA 0
#define NSTAT (2 * DEPTH + 2)
#define PSZ 16
#define CH 32
#define IPF 2
#define RLIM 3
#define RSW 12
#define PA 2
#define WB 1
#define CGROT 1
#define PPRIO 2

typedef __attribute__((ext_vector_type(8))) __bf16 bf16x8;
typedef __attribute__((ext_vector_type(2))) __bf16 bf16x2;
typedef __attribute__((ext_vector_type(4))) float f32x4;
typedef __attribute__((ext_vector_type(2))) float f32x2;
typedef __attribute__((ext_vector_type(4))) unsigned int u32x4;
typedef __attribute__((ext_vector_type(2))) unsigned int u32x2;
typedef __attribute__((address_space(3))) volatile int lds_vint;
typedef __attribute__((address_space(3))) volatile unsigned long long lds_vu64;
typedef __attribute__((address_space(3))) volatile u32x4 lds_vu4;

constexpr int C = 192;
constexpr int ROWB = C * 2;
constexpr int TPX = 16;
constexpr int TILEB = TPX * ROWB;
constexpr int TSL = TILEB / 16;
constexpr unsigned OOB = 0xF0000000u;
constexpr int WSLOTS = 72 * 64;
constexpr int NCONS = 3;
constexpr int RING0 = WSLOTS;
constexpr int OSTG0 = RING0 + 2 * NSLOT * TSL;
constexpr int FLAG0 = OSTG0 + 2 * NCONS * 128;
constexpr int NFLAG = 64;
static_assert(WB >= 1 && WB <= DEPTH && PA >= 1 && PA <= DEPTH && RLIM < NSLOT && RSW >= NSLOT && NSTAT >= 2 * DEPTH + EXTRA && NSLOT <= 8 && NSLOT >= NCONS, "");
constexpr int SPINMAX = 1 << 22;

__device__ __forceinline__ unsigned pack_bf16(float a, float b) {
    f32x2 v = {a, b};
    bf16x2 r = __builtin_convertvector(v, bf16x2);
    return __builtin_bit_cast(unsigned, r);
}
__device__ __forceinline__ float lo_f(unsigned u) { return __builtin_bit_cast(float, u << 16); }
__device__ __forceinline__ float hi_f(unsigned u) { return __builtin_bit_cast(float, u & 0xffff0000u); }
__device__ __forceinline__ void wait_ge(lds_vint* f, int v) {
#pragma unroll 1
    for (int i = 0; i < SPINMAX; ++i) {
        if (__builtin_amdgcn_readfirstlane(*f) >= v) break;
        __builtin_amdgcn_s_sleep(1);
    }
    asm volatile("" ::: "memory");
}

__device__ __forceinline__ void wait_freed(lds_vint* f, int v) {
#pragma unroll 1
    for (int i = 0; i < SPINMAX; ++i) {
        const u32x4 e = *(lds_vu4*)f;
        if (__builtin_amdgcn_readfirstlane(min(min((int)e[0], (int)e[1]), (int)e[2])) >= v) break;
        __builtin_amdgcn_s_sleep(1);
    }
    asm volatile("" ::: "memory");
}

__global__ __launch_bounds__(512, 2)
void dwsep_kernel(const uint16_t* __restrict__ x, const uint16_t* __restrict__ dw,
                  const uint16_t* __restrict__ pw, uint16_t* __restrict__ out,
                  unsigned* __restrict__ qctr, int ntiles, unsigned nbytes
                  )
{
    __shared__ __attribute__((aligned(16))) u32x4 lds[FLAG0 + NFLAG / 4];
    const int tid = threadIdx.x;
    unsigned ipf;
    {
        unsigned long long pc; int len;
        asm volatile("s_getpc_b64 %0\ndwsep_code_a:\n\ts_mov_b32 %1, dwsep_code_b-dwsep_code_a" : "=s"(pc), "=s"(len));
        const __amdgpu_buffer_rsrc_t rc = __builtin_amdgcn_make_buffer_rsrc((void*)pc, 0, len > 0 ? len : 0, 0x00020000);
        ipf = __builtin_amdgcn_raw_buffer_load_b32(rc, (unsigned)(tid - IPF * 64) * 128u, 0, 0);
        __builtin_amdgcn_sched_barrier(0);
    }
    const int lane = tid & 63;
    const int wave = __builtin_amdgcn_readfirstlane(tid >> 6);
    const int team = wave & 1;
    const int p = lane & 15, g = lane >> 4;
    const int r8 = lane >> 3, c8 = lane & 7;
    const unsigned voff = r8 * ROWB + c8 * 16;
    lds_vint* const flags = (lds_vint*)(lds + FLAG0);
    lds_vu64* const full = (lds_vu64*)(flags + team * 28);
    lds_vint* const freed = flags + team * 28 + 16;
    lds_vint* const wready = flags + 56;
    u32x4* const ring = lds + RING0 + team * NSLOT * TSL;

    if (wave < 2) {
        const __amdgpu_buffer_rsrc_t rx = __builtin_amdgcn_make_buffer_rsrc((void*)x, 0, nbytes, 0x00020000);
        const int nq = (gridDim.x + PSZ - 1) / PSZ;
        const int qid = blockIdx.x / PSZ;
        const int tp = 2 * min(PSZ, (int)gridDim.x - qid * PSZ);
        const int ti = (blockIdx.x % PSZ) * 2 + team;
        unsigned* const ctr = qctr + qid * 64;
        if (ti == 0 && lane == 0) __hip_atomic_store(ctr, (unsigned)(NSTAT * tp), __ATOMIC_RELAXED, __HIP_MEMORY_SCOPE_AGENT);
        auto qtile = [&](const unsigned k) __attribute__((always_inline)) -> int {
            const unsigned long long t = (unsigned long long)(k / CH) * (unsigned)(CH * nq) + (unsigned)(qid * CH) + k % CH;
            return t < (unsigned long long)ntiles ? (int)t : -1;
        };
        __builtin_amdgcn_sched_barrier(0);
        u32x4 dwr[3];
#pragma unroll
        for (int j = 0; j < 3; ++j) dwr[j] = *(const u32x4*)(dw + j * 64 + c8 * 8);
        u32x4 xr[DEPTH][6];
        int tl[DEPTH];
        unsigned kq[DEPTH];
        bool pend[DEPTH];
#pragma unroll
        for (int d = 0; d < DEPTH; ++d) {
            tl[d] = qtile((EXTRA + d) * tp + ti);
            kq[d] = 0; pend[d] = false;
            const unsigned so = ((tl[d] >= 0) ? (unsigned)tl[d] * TILEB : OOB) + voff;
#pragma unroll
            for (int u = 0; u < 6; ++u)
                xr[d][u] = __builtin_amdgcn_raw_buffer_load_b128(rx, so + (u / 3) * 8 * ROWB + (u % 3) * 128, 0, LD_AUX);
            if (d == WB - 1) {
                __builtin_amdgcn_sched_barrier(0);
                if (tid < NFLAG) flags[tid] = 0;
                asm volatile("s_waitcnt lgkmcnt(0)\n\ts_barrier" ::: "memory");
                __builtin_amdgcn_sched_barrier(0);
            }
        }
        __builtin_amdgcn_s_setprio(PPRIO);
        float dwf[3][8];
#pragma unroll
        for (int j = 0; j < 3; ++j)
#pragma unroll
            for (int k = 0; k < 4; ++k) {
                dwf[j][2 * k] = lo_f(dwr[j][k]); dwf[j][2 * k + 1] = hi_f(dwr[j][k]);
                asm volatile("" : "+v"(dwf[j][2 * k]), "+v"(dwf[j][2 * k + 1]));
            }
        asm volatile("" :: "v"(ipf));
        const int iw0 = r8 * 8 + (c8 ^ (r8 >> 1));
        const int iw1 = (8 + r8) * 8 + (c8 ^ (4 + (r8 >> 1)));
        int slot = 0, n = 0;
        auto post = [&](const u32x4 (&xs)[6], const int tile) __attribute__((always_inline)) {
            if (n >= NSLOT) wait_freed(freed, n + 1 - ((RLIM && n >= RSW) ? RLIM : NSLOT));
            u32x4* sb = ring + slot * TSL;
#pragma unroll
            for (int u = 0; u < 6; ++u) {
                const int j = u % 3;
                u32x4 v = xs[u];
#pragma unroll
                for (int k = 0; k < 4; ++k)
                    v[k] = pack_bf16(lo_f(v[k]) * dwf[j][2 * k], hi_f(v[k]) * dwf[j][2 * k + 1]);
                sb[j * 128 + ((u / 3) ? iw1 : iw0)] = v;
            }
            asm volatile("" ::: "memory");
            full[slot] = ((unsigned long long)(unsigned)tile << 32) | (unsigned)(n + 1);
            slot = (slot + 1 == NSLOT) ? 0 : slot + 1;
            ++n;
        };
        bool stop = false, live = true;
#pragma unroll 1
        for (int st0 = EXTRA; live; st0 += DEPTH) {
            live = false;
#pragma unroll
            for (int d = 0; d < DEPTH; ++d) {
                const int st = st0 + d;
                if (tl[d] >= 0) post(xr[d], tl[d]);
                __builtin_amdgcn_sched_barrier(0);
                int nt = -1;
                if (st + DEPTH < NSTAT) {
                    nt = qtile((st + DEPTH) * tp + ti);
                } else if (pend[d]) {
                    nt = qtile(__builtin_amdgcn_readfirstlane(kq[d]));
                    if (nt < 0) stop = true;
                }
                const int dp = (d + PA) % DEPTH;
                pend[dp] = (st + PA + DEPTH >= NSTAT) && !stop;
                if (pend[dp] && lane == 0) {
                    int z = 0;
                    asm volatile("" : "+v"(z));
                    kq[dp] = __hip_atomic_fetch_add(ctr + z, 1u, __ATOMIC_RELAXED, __HIP_MEMORY_SCOPE_AGENT);
                }
                __builtin_amdgcn_sched_barrier(0);
                tl[d] = nt;
                live = live || pend[dp] || nt >= 0 || st + PA + DEPTH < NSTAT;
                const unsigned so = ((nt >= 0) ? (unsigned)nt * TILEB : OOB) + voff;
#pragma unroll
                for (int u = 0; u < 6; ++u)
                    xr[d][u] = __builtin_amdgcn_raw_buffer_load_b128(rx, so + (u / 3) * 8 * ROWB + (u % 3) * 128, 0, LD_AUX);
                __builtin_amdgcn_sched_barrier(0);
            }
        }
        asm volatile("dwsep_code_b:");
        if (n >= NSLOT) wait_freed(freed, n + 1 - NSLOT);
        full[slot] = 0xFFFFFFFF00000000ull | (unsigned)(n + 1);
    } else {
        const __amdgpu_buffer_rsrc_t ro = __builtin_amdgcn_make_buffer_rsrc((void*)out, 0, nbytes, 0x00020000);
        const int cg = (((wave - 2) >> 1) + (CGROT ? blockIdx.x >> 3 : 0)) % 3;
        {
            const int tk = tid & 127;
            const int cb = tk >> 3, db = cg * 8 + (tk & 7);
            const int hf = tk & 1;
            const int cb2 = 16 + (tk >> 4), db2 = cg * 8 + ((tk >> 1) & 7);
            u32x4 wr[8];
            u32x2 wh[8];
            asm volatile("s_barrier" ::: "memory");
            __builtin_amdgcn_sched_barrier(0);
#pragma unroll
            for (int j = 0; j < 8; ++j) wr[j] = *(const u32x4*)(pw + (cb * 8 + j) * C + db * 8);
#pragma unroll
            for (int j = 0; j < 8; ++j) wh[j] = *(const u32x2*)(pw + (cb2 * 8 + j) * C + db2 * 8 + hf * 4);
            __builtin_amdgcn_sched_barrier(0);
#pragma unroll
            for (int e = 0; e < 8; ++e) {
                u32x4 v;
                const unsigned sel = (e & 1) ? 0x07060302u : 0x05040100u;
#pragma unroll
                for (int jj = 0; jj < 4; ++jj)
                    v[jj] = __builtin_amdgcn_perm(wr[2 * jj + 1][e >> 1], wr[2 * jj][e >> 1], sel);
                lds[((2 * (db >> 2) + (e >> 2)) * 6 + (cb >> 2)) * 64 + (cb & 3) * 16 + 4 * (db & 3) + (e & 3)] = v;
            }
#pragma unroll
            for (int e = 0; e < 4; ++e) {
                u32x4 v;
                const unsigned sel = (e & 1) ? 0x07060302u : 0x05040100u;
#pragma unroll
                for (int jj = 0; jj < 4; ++jj)
                    v[jj] = __builtin_amdgcn_perm(wh[2 * jj + 1][e >> 1], wh[2 * jj][e >> 1], sel);
                lds[((2 * (db2 >> 2) + hf) * 6 + (cb2 >> 2)) * 64 + (cb2 & 3) * 16 + 4 * (db2 & 3) + e] = v;
            }
            asm volatile("" ::: "memory");
            if (lane == 0) __hip_atomic_fetch_add((int*)(lds + FLAG0) + 56 + cg, 1, __ATOMIC_RELAXED, __HIP_MEMORY_SCOPE_WORKGROUP);
            asm volatile("" :: "v"(ipf));
            wait_ge(wready + cg, 2);
        }
        u32x4 wf[24];
#pragma unroll
        for (int f = 0; f < 24; ++f) wf[f] = lds[(cg * 24 + f) * 64 + lane];
        u32x4* const stg = lds + OSTG0 + (wave - 2) * 128;
        const int ir0 = p * 8 + (g ^ (p >> 1));
        const int ir1 = p * 8 + ((4 + g) ^ (p >> 1));
        const int ow0 = p * 8 + (g ^ (p & 7));
        const int ow1 = p * 8 + ((4 + g) ^ (p & 7));
        const int or0 = r8 * 8 + (c8 ^ r8);
        int slot = 0;
#pragma unroll 1
        for (int n = 1;; ++n) {
            int tile = -1;
#pragma unroll 1
            for (int i = 0; i < SPINMAX; ++i) {
                const unsigned long long e = full[slot];
                if (__builtin_amdgcn_readfirstlane((int)(unsigned)e) == n) { tile = __builtin_amdgcn_readfirstlane((int)(e >> 32)); break; }
                __builtin_amdgcn_s_sleep(1);
            }
            asm volatile("" ::: "memory");
            if (tile < 0) break;
            const u32x4* sb = ring + slot * TSL;
            u32x4 xa[6];
#pragma unroll
            for (int j = 0; j < 3; ++j) { xa[2 * j] = sb[j * 128 + ir0]; xa[2 * j + 1] = sb[j * 128 + ir1]; }
            asm volatile("" ::: "memory");
            freed[cg] = n;
            slot = (slot + 1 == NSLOT) ? 0 : slot + 1;
            const unsigned so = (unsigned)tile * TILEB + voff + cg * 128;
            f32x4 acc[4];
#pragma unroll
            for (int h = 0; h < 4; ++h) acc[h] = (f32x4){0.f, 0.f, 0.f, 0.f};
#pragma unroll
            for (int s = 0; s < 6; ++s)
#pragma unroll
                for (int h = 0; h < 4; ++h)
                    acc[h] = __builtin_amdgcn_mfma_f32_16x16x32_bf16(__builtin_bit_cast(bf16x8, wf[h * 6 + s]),
                                                                     __builtin_bit_cast(bf16x8, xa[s]), acc[h], 0, 0, 0);
#pragma unroll
            for (int m = 0; m < 2; ++m) {
                u32x4 o;
                o[0] = pack_bf16(acc[2 * m][0], acc[2 * m][1]);
                o[1] = pack_bf16(acc[2 * m][2], acc[2 * m][3]);
                o[2] = pack_bf16(acc[2 * m + 1][0], acc[2 * m + 1][1]);
                o[3] = pack_bf16(acc[2 * m + 1][2], acc[2 * m + 1][3]);
                stg[m ? ow1 : ow0] = o;
            }
#pragma unroll
            for (int q = 0; q < 2; ++q) {
                const u32x4 v = stg[q * 64 + or0];
                __builtin_amdgcn_raw_buffer_store_b128(v, ro, so + q * 8 * ROWB, 0, ST_AUX);
            }
        }
    }
}

extern "C" void kernel_launch(void* const* d_in, const int* in_sizes, int n_in,
                              void* d_out, int out_size, void* d_ws, size_t ws_size,
                              hipStream_t stream) {
    const uint16_t* x = (const uint16_t*)d_in[0];
    const uint16_t* dw = (const uint16_t*)d_in[1];
    const uint16_t* pw = (const uint16_t*)d_in[2];
    uint16_t* out = (uint16_t*)d_out;
    const int npix = in_sizes[0] / C;
    const int ntiles = (npix + TPX - 1) / TPX;
    int grid = (ntiles + 1) / 2;
    if (grid > NBLK) grid = NBLK;
    unsigned* qctr = (unsigned*)d_ws + (ws_size >= (size_t)(8u << 20) ? (1 << 20) : 0);
    dwsep_kernel<<<grid, 512, 0, stream>>>(x, dw, pw, out, qctr, ntiles, (unsigned)npix * ROWB);
}
```
